```python
import torch

def _softmax(x, axis=-1):
    return torch.softmax(x, dim=axis)

def _gelu(x):
    return torch.nn.functional.gelu(x, approximate='tanh')

def _silu(x):
    return torch.nn.functional.silu(x)

def _sigmoid(x):
    return torch.sigmoid(x)

def _layernorm(x, axis=-1, eps=1e-05):
    m = x.mean(dim=axis, keepdim=True)
    v = x.var(dim=axis, keepdim=True, unbiased=False)
    return (x - m) / torch.sqrt(v + eps)

def _scatter_add(tgt, idx, src, axis=0):
    return tgt.clone().index_add_(axis, idx, src)

def _segment_sum(src, seg, n):
    out = torch.zeros((n,) + src.shape[1:], dtype=src.dtype)
    return out.index_add_(0, seg, src)

def _pad(x, pw):
    flat = tuple((v for pair in reversed(pw) for v in pair))
    return torch.nn.functional.pad(x, flat)
kernel_name = 'adaLN_cond__gqa_block_cached__mixer_token_mlp__decode'

def setup_inputs():

    _w = lambda a: (lambda t: t.to(torch.bfloat16) if t.is_floating_point() else t)(torch.as_tensor(a))

    def _A_setup():
        B, Sq, D = (2, 1, 1024)
        Hq, Hkv, Sk, Dh = (32, 4, 32768, 128)
        assert Hq % Hkv == 0, (Hq, Hkv)
        return {'x': _w((lambda *s: torch.randn(*s, dtype=torch.bfloat16) * 0.05)(B, Sq, D)), 'k_cache': _w((lambda *s: torch.randn(*s, dtype=torch.bfloat16) * 3.2)(B, Hkv, Sk, Dh)), 'v_cache': _w((lambda *s: torch.randn(*s, dtype=torch.bfloat16) * 1.6)(B, Hkv, Sk, Dh)), 'wq': _w((lambda *s: torch.randn(*s, dtype=torch.bfloat16) * 3.2)(D, Hq * Dh) * D ** (-0.5)), 'wo': _w((lambda *s: torch.randn(*s, dtype=torch.bfloat16) * 1.6)(Hq * Dh, D) * (Hq * Dh) ** (-0.5))}
    _A_in = _A_setup()

    def _B_setup():
        B, S, D, F = (2, 1, 1024, 3584)
        return {'x': _w((lambda *s: torch.randn(*s, dtype=torch.bfloat16) * 0.05)(B, S, D)), 'w1': _w((lambda *s: torch.randn(*s, dtype=torch.bfloat16) * 1.6)(S, F) * S ** (-0.5)), 'b1': _w((lambda *s: torch.randn(*s, dtype=torch.bfloat16) * 1.6)(F) * 0.02), 'w2': _w((lambda *s: torch.randn(*s, dtype=torch.bfloat16) * 1.6)(F, S) * F ** (-0.5)), 'b2': _w((lambda *s: torch.randn(*s, dtype=torch.bfloat16) * 1.6)(S) * 0.02)}
    _B_in = _B_setup()
    return {'A_x': _A_in['x'], 'A_k_cache': _A_in['k_cache'], 'A_v_cache': _A_in['v_cache'], 'A_wq': _A_in['wq'], 'A_wo': _A_in['wo'], 'B_x': _B_in['x'], 'B_w1': _B_in['w1'], 'B_b1': _B_in['b1'], 'B_w2': _B_in['w2'], 'B_b2': _B_in['b2'], 'c': _w((lambda *s: torch.randn(*s, dtype=torch.bfloat16) * 0.05)(2, 1024) * 8.0), 'w_mod': _w((lambda *s: torch.randn(*s, dtype=torch.bfloat16) * 0.05)(1024, 6 * 1024) * 1024 ** (-0.5) * 8.0)}

def reference(A_x, A_k_cache, A_v_cache, A_wq, A_wo, B_x, B_w1, B_b1, B_w2, B_b2, c, w_mod):

    def _A_ref(x, k_cache, v_cache, wq, wo):
        B, Sq, D = x.shape
        Hq, Dh = (32, 128)
        Hkv = k_cache.shape[1]
        rep = Hq // Hkv
        q = (lambda x, *d: x.permute(*d))((x @ wq).reshape(B, Sq, Hq, Dh), 0, 2, 1, 3)
        k = (lambda x, n, axis: torch.repeat_interleave(x, n, dim=axis))(k_cache, rep, axis=1)
        v = (lambda x, n, axis: torch.repeat_interleave(x, n, dim=axis))(v_cache, rep, axis=1)
        scores = torch.einsum('bhqd,bhkd->bhqk', q, k) * Dh ** (-0.5)
        p = _softmax(scores, axis=-1)
        o = torch.einsum('bhqk,bhkd->bhqd', p, v)
        return (lambda x, *d: x.permute(*d))(o, 0, 2, 1, 3).reshape(B, Sq, Hq * Dh) @ wo

    def _B_ref(x, w1, b1, w2, b2):
        B, S, D = x.shape
        h = _layernorm(x, axis=-1)
        u = torch.einsum('bsd,sf->bdf', h, w1) + b1
        u = _gelu(u)
        y = torch.einsum('bdf,fs->bds', u, w2) + b2
        return (lambda x, *d: x.permute(*d))(y, 0, 2, 1)
    x = A_x
    mod = (_silu(c) @ w_mod).reshape(c.shape[0], 6, 1, -1)
    sa, ca, ga, sb, cb, gb = (mod[:, 0].clone(), mod[:, 1].clone(), mod[:, 2].clone(), mod[:, 3].clone(), mod[:, 4].clone(), mod[:, 5].clone())
    h = _layernorm(x, axis=-1) * (1 + ca) + sa
    x = x + ga * _A_ref(h, A_k_cache, A_v_cache, A_wq, A_wo)
    h = _layernorm(x, axis=-1) * (1 + cb) + sb
    x = x + gb * _B_ref(h, B_w1, B_b1, B_w2, B_b2)
    return x
```

```cpp
#include <hip/hip_runtime.h>
#include <hip/hip_bf16.h>
#include <type_traits>

typedef unsigned short u16;
typedef unsigned long long u64;
typedef unsigned v4u __attribute__((ext_vector_type(4)));
typedef unsigned v2u __attribute__((ext_vector_type(2)));
typedef float v4f __attribute__((ext_vector_type(4)));
typedef float v2f __attribute__((ext_vector_type(2)));
typedef __bf16 v2bf __attribute__((ext_vector_type(2)));
typedef __bf16 v4bf __attribute__((ext_vector_type(4)));
typedef __bf16 v8bf __attribute__((ext_vector_type(8)));
#define LAS __attribute__((address_space(3)))
#define GAS __attribute__((address_space(1)))

constexpr int NWG = 256, NTHR = 640;
constexpr int NT = 32;
constexpr int NF0 = 12, NEV = 10 + 2 * NT;
constexpr int NSLOT = 18;
constexpr int WIN = 24;
constexpr int TAU = 464;
constexpr int EA = 26;
constexpr int EB = 38;
constexpr int HOLDH = 10;
constexpr int HOLDQ = 4;
constexpr int FSL = 24;
constexpr int WIN2 = 48;
constexpr int LATE = 60;
constexpr int SLH = 24;
constexpr int SLQ = 20;
constexpr int WOD = 36;
constexpr int S1A = 14;
constexpr int TSPLIT = 4;
constexpr int WNT = 0;
constexpr unsigned SENT = 0xFFFFFFFFu;
constexpr int TAB_PER = 20, TAB_HALF = 2560, TAB_BASE = 111 << 7;

constexpr int WS_EPOCH = 0;
constexpr int WS_CL = 2048;
constexpr int WS_MR = 4096;
constexpr int WS_OS = 20480;
constexpr int WS_TAB = 36864;
constexpr int WS_MP = 131072;
constexpr int WS_QP = 262144;
constexpr int WS_RP = 393216;
constexpr int WS_XP = 524288;
constexpr int WS_PART = 1048576;
constexpr int WS_END = WS_PART + 1048576;

constexpr int L_RING = 0;
constexpr int L_TAB = NSLOT * 8192;
constexpr int L_C = L_TAB, L_X = L_TAB + 4096, L_E = L_TAB + 8192;
constexpr int L_Q = L_TAB + 10240;
constexpr int L_H = L_Q + 2048;
constexpr int L_ST = L_H + 1024;
constexpr int L_REDT = L_ST + 512;
constexpr int L_CTRL = L_REDT + 512;
constexpr int L_PROG = L_CTRL;
constexpr int L_HALV = L_CTRL + 32;
constexpr int L_CNTH = L_CTRL + 36, L_CNTG = L_CTRL + 40, L_CNTT = L_CTRL + 44, L_CNTC = L_CTRL + 48;
constexpr int L_FO = L_CTRL + 52, L_FC = L_CTRL + 60, L_HOLD = L_CTRL + 64, L_TA = L_CTRL + 68, L_BASE = L_CTRL + 72;
constexpr int L_BYTES = L_CTRL + 128;
static_assert(L_BYTES <= 163840, "LDS");

constexpr unsigned SPIN_G = 1u << 16;
constexpr unsigned SPIN_L = 1u << 22;

__device__ __forceinline__ float lo16(unsigned u) { return __uint_as_float(u << 16); }
__device__ __forceinline__ float hi16(unsigned u) { return __uint_as_float(u & 0xffff0000u); }
__device__ __forceinline__ unsigned pk2(float a, float b) { v2f z = {a, b}; return __builtin_bit_cast(unsigned, __builtin_convertvector(z, v2bf)); }
__device__ __forceinline__ float rb(float a) { return lo16(pk2(a, a)); }
__device__ __forceinline__ v2f up2(unsigned u) { return (v2f){lo16(u), hi16(u)}; }
__device__ __forceinline__ v2f rbv(v2f a) { return up2(pk2(a[0], a[1])); }
__device__ __forceinline__ unsigned nosent(unsigned u) { return u == SENT ? 0x7FC07FC0u : u; }
template <int C> __device__ __forceinline__ unsigned dppu(unsigned x) { return (unsigned)__builtin_amdgcn_mov_dpp((int)x, C, 0xf, 0xf, true); }
template <int C> __device__ __forceinline__ float dppf(float x) { return __uint_as_float(dppu<C>(__float_as_uint(x))); }
constexpr int XOR1 = 0xB1, XOR2 = 0x4E, ROR4 = 0x124, ROR8 = 0x128;
__device__ __forceinline__ float red32(float a, float b) {
  auto s = __builtin_amdgcn_permlane32_swap(__float_as_uint(a), __float_as_uint(b), false, false);
  return __uint_as_float(s[0]) + __uint_as_float(s[1]);
}
__device__ __forceinline__ float red16(float a, float b) {
  auto s = __builtin_amdgcn_permlane16_swap(__float_as_uint(a), __float_as_uint(b), false, false);
  return __uint_as_float(s[0]) + __uint_as_float(s[1]);
}
__device__ __forceinline__ float fmx(float a, float b) {
  float r;
  asm("v_max_f32 %0, %1, %2" : "=v"(r) : "v"(a), "v"(b));
  return r;
}
__device__ __forceinline__ float fmx3(float a, float b, float c) {
  float r;
  asm("v_max3_f32 %0, %1, %2, %3" : "=v"(r) : "v"(a), "v"(b), "v"(c));
  return r;
}
__device__ __forceinline__ float max8(float a) {
  float r;
  asm("s_nop 1\n\tv_max_f32_dpp %0, %1, %1 row_ror:8 row_mask:0xf bank_mask:0xf" : "=v"(r) : "v"(a));
  return r;
}
__device__ __forceinline__ float max32(float a) {
  auto s = __builtin_amdgcn_permlane32_swap(__float_as_uint(a), __float_as_uint(a), false, false);
  return fmx(__uint_as_float(s[0]), __uint_as_float(s[1]));
}
__device__ __forceinline__ float max16(float a) {
  auto s = __builtin_amdgcn_permlane16_swap(__float_as_uint(a), __float_as_uint(a), false, false);
  return fmx(__uint_as_float(s[0]), __uint_as_float(s[1]));
}
__device__ __forceinline__ float allsum_b15(float x) {
  x = red32(x, x); x = red16(x, x); x += dppf<ROR8>(x); x += dppf<ROR4>(x); x += dppf<XOR2>(x); return x;
}
__device__ __forceinline__ float allsum64(float x) { x = allsum_b15(x); x += dppf<XOR1>(x); return x; }
__device__ __forceinline__ float ex2(float x) { return __builtin_amdgcn_exp2f(x); }
__device__ __forceinline__ float rcp(float x) { return __builtin_amdgcn_rcpf(x); }
#define CBAR() asm volatile("" ::: "memory")
template <typename T> __device__ __forceinline__ void pin(T& v) { asm volatile("" : "+v"(v)); }
template <typename T, int N> __device__ __forceinline__ void pin(T (&v)[N]) {
#pragma unroll
  for (int i = 0; i < N; ++i) asm volatile("" : "+v"(v[i]));
}
#define WAIT_LGKM0() asm volatile("s_waitcnt lgkmcnt(0)" ::: "memory")
template <int N> __device__ __forceinline__ void wait_vm() { asm volatile("s_waitcnt vmcnt(%0)" ::"n"(N) : "memory"); }

typedef __amdgpu_buffer_rsrc_t rsrc_t;
__device__ __forceinline__ rsrc_t mk_rsrc(const void* p, unsigned bytes) { return __builtin_amdgcn_make_buffer_rsrc((void*)p, 0, bytes, 0x00020000); }
constexpr int AUX_SC1 = 16;
template <int AUX> __device__ __forceinline__ v4u ld128(rsrc_t r, unsigned vo, unsigned so = 0) { return __builtin_amdgcn_raw_buffer_load_b128(r, vo, so, AUX); }
template <int AUX> __device__ __forceinline__ unsigned ld32(rsrc_t r, unsigned vo, unsigned so = 0) { return __builtin_amdgcn_raw_buffer_load_b32(r, vo, so, AUX); }
__device__ __forceinline__ void st128(rsrc_t r, v4u v, unsigned vo) { __builtin_amdgcn_raw_buffer_store_b128(v, r, vo, 0, AUX_SC1); }
__device__ __forceinline__ void st64(rsrc_t r, v2u v, unsigned vo) { __builtin_amdgcn_raw_buffer_store_b64(v, r, vo, 0, AUX_SC1); }
__device__ __forceinline__ void st32(rsrc_t r, unsigned v, unsigned vo) { __builtin_amdgcn_raw_buffer_store_b32(v, r, vo, 0, AUX_SC1); }

#define DMA8_BODY(P)                                                        \
  asm volatile(                                                             \
      "s_mov_b32 m0, %0\n\ts_nop 0\n\t"                                     \
      "global_load_lds_dwordx4 %2, %1" P "\n\t"                             \
      "global_load_lds_dwordx4 %3, %1 offset:1024" P "\n\t"                 \
      "global_load_lds_dwordx4 %4, %1 offset:2048" P "\n\t"                 \
      "global_load_lds_dwordx4 %5, %1 offset:3072" P "\n\t"                 \
      "s_add_u32 m0, m0, 0x1000\n\ts_nop 0\n\t"                             \
      "global_load_lds_dwordx4 %6, %1" P "\n\t"                             \
      "global_load_lds_dwordx4 %7, %1 offset:1024" P "\n\t"                 \
      "global_load_lds_dwordx4 %8, %1 offset:2048" P "\n\t"                 \
      "global_load_lds_dwordx4 %9, %1 offset:3072" P                        \
      ::"s"(lds), "s"(gb), "v"(o0), "v"(o1), "v"(o2), "v"(o3), "v"(o4), "v"(o5), "v"(o6), "v"(o7) : "memory", "scc")
template <bool NTP>
__device__ __forceinline__ void dma8(unsigned lds, const char* gb, unsigned o0, unsigned o1, unsigned o2, unsigned o3,
                                     unsigned o4, unsigned o5, unsigned o6, unsigned o7) {
  if constexpr (NTP) DMA8_BODY(" nt"); else DMA8_BODY("");
}


struct Params {
  const char *x, *K, *V, *wq, *wo, *w1, *b1, *w2, *b2, *c, *wm;
  char* out;
  char* ws;
};

template <int NF, int NS = (NF > 8 ? 8 : NF)>
__device__ __forceinline__ float gemv_mfma(const v8bf* wf, v4u av, int lane, v4u av2 = v4u{}) {
  v4f d[NS];
#pragma unroll
  for (int j = 0; j < NS; ++j) d[j] = __builtin_amdgcn_mfma_f32_16x16x32_bf16(__builtin_bit_cast(v8bf, av), wf[j], (v4f){0.f, 0.f, 0.f, 0.f}, 0, 0, 0);
#pragma unroll
  for (int j = NS; j < NF; ++j) d[j - NS] = __builtin_amdgcn_mfma_f32_16x16x32_bf16(__builtin_bit_cast(v8bf, av2), wf[j], d[j - NS], 0, 0, 0);
  const bool g1 = lane & 16, g2 = lane & 32;
  float s[2];
#pragma unroll
  for (int b = 0; b < 2; ++b) {
    const float lo = (g1 ? d[2][b] : d[0][b]) + (g1 ? d[3][2 + b] : d[1][2 + b]);
    if constexpr (NS == 8) s[b] = g2 ? (g1 ? d[6][b] : d[4][b]) + (g1 ? d[7][2 + b] : d[5][2 + b]) : lo;
    else s[b] = g2 ? 0.f : lo;
  }
  const float t = red32(s[0], s[1]);
  return red16(t, t);
}
__device__ __forceinline__ void publish_part(float d, rsrc_t rws, unsigned off, unsigned t4, int w, int lane) {
  const unsigned u = ((__float_as_uint(d) + 8u) & ~15u) | t4;
  const v4u v = {u, dppu<0x55>(u), dppu<0xAA>(u), dppu<0xFF>(u)};
  if (!(lane & 19)) st128(rws, v, off + (lane >> 5) * 256 + w * 64 + (lane & 15) * 4);
}
template <int N, int STRIDE, int HOLD, typename F>
__device__ __forceinline__ void gather_tagged(v4u (&ps)[N], rsrc_t rws, unsigned off, unsigned t4, unsigned& bg, LAS unsigned* hold, int lane, F meanwhile) {
  unsigned pend = (1u << N) - 1;
  if (HOLD) {
    if (lane == 0) __hip_atomic_fetch_add(hold, 1u, __ATOMIC_RELAXED, __HIP_MEMORY_SCOPE_WORKGROUP);
    __builtin_amdgcn_s_sleep(HOLD);
  }
  CBAR();
#pragma unroll
  for (int i = 0; i < N; ++i) ps[i] = ld128<AUX_SC1>(rws, off, i * STRIDE);
  CBAR();
  if (HOLD && lane == 0) __hip_atomic_fetch_add(hold, ~0u, __ATOMIC_RELAXED, __HIP_MEMORY_SCOPE_WORKGROUP);
  meanwhile();
  for (;; --bg) {
#pragma unroll
    for (int i = 0; i < N; ++i) {
      const unsigned d = ((ps[i].x ^ t4) | (ps[i].y ^ t4)) | ((ps[i].z ^ t4) | (ps[i].w ^ t4));
      if (__all((d & 15u) == 0)) pend &= ~(1u << i);
    }
    if (pend == 0 || bg == 0) break;
    __builtin_amdgcn_s_sleep(2);
    CBAR();
#pragma unroll
    for (int i = 0; i < N; ++i) if (pend & (1u << i)) ps[i] = ld128<AUX_SC1>(rws, off, i * STRIDE);
  }
#pragma unroll
  for (int i = 0; i < N; ++i) ps[i] &= ~15u;
}
__device__ __forceinline__ unsigned umax4(v4u v) { return max(max(v.x, v.y), max(v.z, v.w)); }
__device__ __forceinline__ bool arrive(LAS char* L, int off, unsigned n, int lane) {
  WAIT_LGKM0();
  unsigned old = 0;
  if (lane == 0) old = __hip_atomic_fetch_add((LAS unsigned*)(L + off), 1u, __ATOMIC_RELAXED, __HIP_MEMORY_SCOPE_WORKGROUP);
  old = __builtin_amdgcn_readfirstlane(old);
  CBAR();
  return old == n - 1;
}
__device__ __forceinline__ unsigned next_tag(unsigned e) { return e * 0x9E3779B1u + 0x7F4A7C15u; }
constexpr float LOG2E = 1.4426950408889634f;
struct Unit { float c3, c2, c1, c0, e1, e0; };
__device__ __forceinline__ Unit mk_unit(float a, float b, float c) {
  constexpr float KA = -2.f * 0.7978845608028654f * LOG2E, KB = KA * 0.044715f;
  return {KB * a * a * a, 3.f * KB * a * a * b, a * fmaf(3.f * KB * b, b, KA), b * fmaf(KB * b, b, KA), a * c, b * c};
}
__device__ __forceinline__ v2f unit_acc(const Unit& u, v2f t, v2f s) {
  v2f z = __builtin_elementwise_fma(t, (v2f)(u.c3), (v2f)(u.c2));
  z = __builtin_elementwise_fma(z, t, (v2f)(u.c1));
  z = __builtin_elementwise_fma(z, t, (v2f)(u.c0));
  const v2f d = (v2f){ex2(z[0]), ex2(z[1])} + 1.f;
  const v2f r = {rcp(d[0]), rcp(d[1])};
  return __builtin_elementwise_fma(__builtin_elementwise_fma(t, (v2f)(u.e1), (v2f)(u.e0)), r, s);
}

__global__ void __launch_bounds__(NTHR, 3) fused_block(Params p) {
  __shared__ __attribute__((aligned(16))) char lds[L_BYTES];
  LAS char* L = (LAS char*)lds;
  const char* code0;
  unsigned code_a, code_b;
  asm volatile("s_getpc_b64 %0\nfb_code0:\n\ts_mov_b32 %1, fb_code1-fb_code0\n\ts_mov_b32 %2, fb_code2-fb_code0" : "=s"(code0), "=s"(code_a), "=s"(code_b));
  const int tid = threadIdx.x, lane = tid & 63;
  const int wave = __builtin_amdgcn_readfirstlane(tid >> 6);
  const int xid = (blockIdx.x & 7) * 32 + (blockIdx.x >> 3), bid = xid;
  unsigned bl = SPIN_L, bg = SPIN_G;
  auto lw = [&](int off) { return (volatile LAS unsigned*)(L + off); };
  auto wait_l = [&](int off, unsigned v, auto sl) {
    for (; bl; --bl) { if (__builtin_amdgcn_readfirstlane(*lw(off)) >= v) break; if (sl) __builtin_amdgcn_s_sleep(decltype(sl)::value); }
    CBAR();
  };
  constexpr std::integral_constant<int, 0> S0{};
  constexpr std::integral_constant<int, 1> S1{};
  constexpr std::integral_constant<int, 4> S4{};
  if (wave == 0) {
    const unsigned top = (max(code_a, code_b) - 16) & ~3u, o1 = min((blockIdx.x >> 3) * 1024u + lane * 16, top), o2 = min(o1 + 32768, top);
    asm volatile("s_mov_b32 m0, %0\n\ts_nop 0\n\tglobal_load_lds_dwordx4 %2, %1\n\tglobal_load_lds_dwordx4 %3, %1"
                 ::"s"((unsigned)(size_t)(L + L_Q)), "s"(code0), "v"(o1), "v"(o2) : "memory");
  }
  if (tid < 32) *(LAS unsigned*)(L + L_CTRL + tid * 4) = 0u;
  asm volatile("" ::"s"(p.x), "s"(p.K), "s"(p.V), "s"(p.wq), "s"(p.wo), "s"(p.w1), "s"(p.b1), "s"(p.w2), "s"(p.b2), "s"(p.c),
               "s"(p.wm), "s"(p.out), "s"(p.ws));
  const unsigned lbase = (unsigned)(size_t)(L + L_RING);
  if (wave == 0) {
    asm volatile(
        "s_mov_b32 m0, %0\n\ts_nop 0\n\t"
        "global_load_lds_dwordx4 %4, %1\n\tglobal_load_lds_dwordx4 %4, %1 offset:1024\n\t"
        "global_load_lds_dwordx4 %4, %1 offset:2048\n\tglobal_load_lds_dwordx4 %4, %1 offset:3072\n\t"
        "s_add_u32 m0, m0, 0x2000\n\ts_nop 0\n\t"
        "global_load_lds_dwordx4 %4, %3 sc1\n\t"
        "s_sub_u32 m0, m0, 0x1000\n\ts_nop 0\n\t"
        "global_load_lds_dwordx4 %4, %2\n\tglobal_load_lds_dwordx4 %4, %2 offset:1024\n\t"
        "global_load_lds_dwordx4 %4, %2 offset:2048\n\tglobal_load_lds_dwordx4 %4, %2 offset:3072"
        ::"s"(lbase + L_C), "s"(p.c), "s"(p.x), "s"(p.ws), "v"(lane * 16) : "memory", "scc");
    const char* wb = p.wm + (size_t)(128 * (xid >> 5)) * 12288 + (xid & 31) * 128;
    const unsigned o0 = (lane >> 3) * 12288 + (lane & 7) * 16;
    constexpr unsigned D = 8 * 12288 - 1024, D4 = 32 * 12288;
    dma8<WNT>(lbase, wb, o0, o0 + D, o0 + 2 * D, o0 + 3 * D, o0 + D4, o0 + D4 + D, o0 + D4 + 2 * D, o0 + D4 + 3 * D);
    dma8<WNT>(lbase + 8192, wb + 64 * 12288, o0, o0 + D, o0 + 2 * D, o0 + 3 * D, o0 + D4, o0 + D4 + D, o0 + D4 + 2 * D, o0 + D4 + 3 * D);
  }
  WAIT_LGKM0(); __builtin_amdgcn_s_barrier(); CBAR();

  if (__builtin_expect(wave == 0, 1)) {
    __builtin_amdgcn_s_setprio(2);
    const char* kb = p.K + (size_t)bid * (NT * 8192);
    const char* vb = p.V + (size_t)bid * (NT * 8192);
    const unsigned r4 = lane >> 4, pos = lane & 15;
    const unsigned k0 = r4 * 256 + ((pos ^ r4) * 16);
    const unsigned v0 = r4 * 256 + ((pos ^ (r4 << 1)) * 16);

    unsigned e = 2, relA = 0, relB = 0;
    auto wait_rel = [&](int off, unsigned v, unsigned& rel) {
      if (rel < v)
        for (; bl; --bl) {
          const v4u pr = *(volatile LAS v4u*)(L + off);
          rel = __builtin_amdgcn_readfirstlane(min(min(pr.x, pr.y), min(pr.z, pr.w)));
          if (rel >= v) break;
          __builtin_amdgcn_s_sleep(1);
        }
      CBAR();
    };
    bool behind = false;
    auto landed = [&](auto early) {
      ++e;
      if (!decltype(early)::value && behind) { wait_vm<WIN2>(); *lw(L_HALV) = e - WIN2 / 8; }
      else { wait_vm<WIN>(); if (e > WIN / 8) *lw(L_HALV) = e - WIN / 8; }
      if (decltype(early)::value) for (; bl && __builtin_amdgcn_readfirstlane(*lw(L_HOLD)); --bl) {}
    };
    constexpr std::true_type W0{};
    constexpr std::false_type W1{};
    unsigned o[8];
    const char* wb;
    unsigned wst;
    auto tile = [&](const char* b, unsigned st) {
      wb = b; wst = 64 * st;
#pragma unroll
      for (int j = 0; j < 8; ++j) o[j] = (8 * j + (lane >> 3)) * st + (lane & 7) * 16 - 1024 * (j & 3);
    };
    auto ev_w = [&](int slot) { dma8<WNT>(lbase + slot * 8192, wb, o[0], o[1], o[2], o[3], o[4], o[5], o[6], o[7]); wb += wst; };
    unsigned ls = lbase + 6 * 8192;
    auto adv = [&]() { ls = (ls == lbase + (NSLOT - 1) * 8192) ? lbase : ls + 8192; };
    auto ev_k = [&](auto infl) { dma8<true>(ls, kb, k0, k0 ^ 64, k0 ^ 128, k0 ^ 192, k0 + 4096, (k0 ^ 64) + 4096, (k0 ^ 128) + 4096, (k0 ^ 192) + 4096); kb += 8192; adv(); landed(infl); };
    auto ev_v = [&](auto infl) { dma8<true>(ls, vb, v0, v0 ^ 128, v0, v0 ^ 128, v0 + 4096, (v0 ^ 128) + 4096, v0 + 4096, (v0 ^ 128) + 4096); vb += 8192; adv(); landed(infl); };
    static_assert(WIN == 24 && NSLOT == 18 && NF0 == 12, "slot plan");
    tile(p.wq + (size_t)(256 * (xid >> 6)) * 8192 + ((xid ^ 1) & 63) * 128, 8192);
#pragma unroll 1
    for (int i = 2; i < 6; ++i) { ev_w(i); landed(W0); }
    ev_k(W0); ev_k(W0);
#pragma unroll 1
    for (int t = 0; t < 5; ++t) { ev_k(W0); ev_v(W0); }
    wait_rel(L_PROG, 6, relA);
    tile(p.wm + (size_t)(256 * (xid >> 6)) * 12288 + 4096 + ((xid ^ 2) & 63) * 128, 12288);
#pragma unroll 1
    for (int i = 2; i < 6; ++i) { ev_w(i); landed(W0); }
    ev_k(W0); ev_v(W0);
    wait_rel(L_PROG + 16, 1, relB); ev_k(W0);
    wait_rel(L_PROG + 16, 2, relB); ev_v(W0);
    wait_rel(L_PROG + 16, 3, relB); ev_k(W0);
    wait_rel(L_PROG + 16, 4, relB); ev_v(W0);
    unsigned base = 0;
    auto pace = [&]() {
      const unsigned el = e - WIN / 8;
      if (base == 0) {
        if (el == EA) *lw(L_TA) = (unsigned)__builtin_amdgcn_s_memrealtime() | 1u;
        if (el > EB) base = __builtin_amdgcn_readfirstlane(*lw(L_BASE));
      } else {
        const unsigned due = base + (el - EA) * TAU;
        int d = (int)((unsigned)__builtin_amdgcn_s_memrealtime() * 16u - due);
        behind = d > LATE * 16;
        for (; bl && d < 0; --bl) { __builtin_amdgcn_s_sleep(1); d = (int)((unsigned)__builtin_amdgcn_s_memrealtime() * 16u - due); }
      }
    };
#pragma unroll 1
    for (int f = NSLOT; f < 2 * NT - 2; f += 2) {
      wait_rel(L_PROG, f - 11, relA); ev_k(W1); pace();
      wait_rel(L_PROG, f - 10, relA); ev_v(W1); pace();
    }
    wait_rel(L_PROG, 2 * NT - 13, relA); ev_v(W1);
    wait_rel(L_PROG, 2 * NT - 12, relA); ev_v(W1);
    auto drain = [&](unsigned last) {
      wait_vm<40>(); *lw(L_HALV) = last - 5;
      wait_vm<32>(); *lw(L_HALV) = last - 4;
      wait_vm<24>(); *lw(L_HALV) = last - 3;
      wait_vm<16>(); *lw(L_HALV) = last - 2;
      wait_vm<8>(); *lw(L_HALV) = last - 1;
      wait_vm<0>(); *lw(L_HALV) = last;
    };
    unsigned last = NEV;
    if (xid & 1) {
      tile(p.wo + (size_t)(512 * (xid >> 5)) * 2048 + ((xid >> 1) & 15) * 128, 2048);
      wait_rel(L_PROG, 2 * NT - 4, relA);
      if (behind) { wait_vm<40>(); *lw(L_HALV) = NEV - 5; wait_vm<32>(); *lw(L_HALV) = NEV - 4; }
#pragma unroll 1
      for (int i = 0; i < 8; ++i) {
        if (i < 4) { wait_vm<WIN>(); *lw(L_HALV) = NEV + i - WIN / 8; }
        if (i == 4) __builtin_amdgcn_s_sleep(WOD);
        wait_vm<WIN2>();
        dma8<WNT>(ls, wb, o[0], o[1], o[2], o[3], o[4], o[5], o[6], o[7]);
        wb += wst; adv();
      }
      last = NEV + 8;
    }
    drain(last);
    asm volatile("fb_code1:");
    return;
  }

  const rsrc_t rws = mk_rsrc(p.ws, WS_END);
  if (wave == 9) {
    wait_l(L_CNTC, 4, S4);
    const unsigned tag = next_tag(__builtin_amdgcn_readfirstlane(*(LAS unsigned*)(L + L_E)));
    wait_l(L_TA, 1, S4);
    const unsigned ta0 = __builtin_amdgcn_readfirstlane(*lw(L_TA));
    if (lane == 0) st64(rws, (v2u){ta0, tag}, WS_CL + bid * 8);
    wait_l(L_HALV, EB, S4);
    v4u v0, v1;
    for (;; --bg) {
      CBAR();
      v0 = ld128<AUX_SC1>(rws, WS_CL + lane * 16); v1 = ld128<AUX_SC1>(rws, WS_CL + 1024 + lane * 16);
      if (__all(v0.y == tag && v0.w == tag && v1.y == tag && v1.w == tag) || bg == 0) break;
      __builtin_amdgcn_s_sleep(8);
    }
    const float mean = allsum64((float)((int)(v0.x - ta0) + (int)(v0.z - ta0) + (int)(v1.x - ta0) + (int)(v1.z - ta0))) * (16.f / 256);
    *lw(L_BASE) = (ta0 * 16u + min(max((int)mean, -16000), 16000)) | 1u;
    *lw(L_FC) = 1u;
    if (bid == 0 && lane == 0) st32(rws, tag, WS_EPOCH);
    return;
  }
  const rsrc_t rx = mk_rsrc(p.x, 4096);
  const rsrc_t rw1 = mk_rsrc(p.w1, 7168), rb1 = mk_rsrc(p.b1, 7168), rw2 = mk_rsrc(p.w2, 7168), rb2 = mk_rsrc(p.b2, 2);
  const int ww = wave - 1;
  const int w = ww & 3;
  const int grp = bid >> 5;
  const int half = lane & 1;
  const bool fin = (bid & 127) == 0;
  const int bb = bid >> 7;
  const int g = lane >> 4, n = lane & 15;
  unsigned avail = 0;
  auto need_ev = [&](unsigned v) {
    if (avail < v)
      for (; bl; --bl) { avail = __builtin_amdgcn_readfirstlane(*lw(L_HALV)); if (avail >= v) break; }
    CBAR();
  };
  auto read_frags = [&](v8bf& f0, v8bf& f1, int e, int slot, int rel) {
    need_ev(e + 1);
    LAS char* S = L + L_RING + slot * 8192 + (8 * g + ((lane >> 2) & 3)) * 128 + w * 32 + (lane & 3) * 8;
    const v4bf t0 = __builtin_amdgcn_ds_read_tr16_b64_v4bf16((LAS v4bf*)(S));
    const v4bf t1 = __builtin_amdgcn_ds_read_tr16_b64_v4bf16((LAS v4bf*)(S + 512));
    const v4bf t2 = __builtin_amdgcn_ds_read_tr16_b64_v4bf16((LAS v4bf*)(S + 4096));
    const v4bf t3 = __builtin_amdgcn_ds_read_tr16_b64_v4bf16((LAS v4bf*)(S + 4096 + 512));
    f0 = __builtin_shufflevector(t0, t1, 0, 1, 2, 3, 4, 5, 6, 7); f1 = __builtin_shufflevector(t2, t3, 0, 1, 2, 3, 4, 5, 6, 7);
    asm volatile("" : "+v"(f0), "+v"(f1)::"memory");
    *lw(L_PROG + ww * 4) = rel;
  };
  const int nA = 128 * ww + 8 * (lane & 15) + 4 * ((lane >> 4) & 1) + 2 * (lane >> 5);
  unsigned mr[4] = {SENT, SENT, SENT, SENT}, xp = 0;
  auto load_mr = [&]() {
#pragma unroll
    for (int i = 0; i < 4; ++i) mr[i] = ld32<AUX_SC1>(rws, WS_MR + bb * 8192 + nA * 2 + 2048 * i);
    xp = ld32<0>(rx, bb * 2048 + nA * 2);
  };
  unsigned sto = L_ST + ww * 64 + ((lane & 15) == 0 ? ((g >> 1) + 2 * (g & 1)) * 4 : (4 + (g >> 1)) * 4);
  auto wave_sums = [&](float q0, float q1, float q2, float q3, float q4, float q5, int off) {
    float a0 = red16(red32(q0, q1), red32(q2, q3)), a1 = red32(q4, q5);
    a1 = red16(a1, a1);
    a0 += dppf<ROR8>(a0); a1 += dppf<ROR8>(a1); a0 += dppf<ROR4>(a0); a1 += dppf<ROR4>(a1);
    a0 += dppf<XOR2>(a0); a1 += dppf<XOR2>(a1); a0 += dppf<XOR1>(a0); a1 += dppf<XOR1>(a1);
    if ((lane & 15) == 0 || (lane & 31) == 1) *(LAS float*)(L + sto + off) = (lane & 1) ? a1 : a0;
  };
  v2f ga = {0.f, 0.f}, sb = ga, cb = ga, gb = ga, xv = ga, cc = ga, cs = ga;
  auto final_prep = [&]() {
    for (; bg; --bg) {
      if (__all(max(max(mr[0], mr[1]), max(mr[2], mr[3])) != SENT)) break;
      CBAR();
      load_mr();
    }
    ga = up2(mr[0]); sb = up2(mr[1]); cb = rbv(up2(mr[2]) + 1.f); gb = up2(mr[3]); xv = up2(xp);
    cc = cb * cb; cs = cb * sb;
    wave_sums(cb[0] + cb[1], sb[0] + sb[1], cc[0] + cc[1], cs[0] + cs[1], sb[0] * sb[0] + sb[1] * sb[1], 0.f, 32);
    pin(ga); pin(sb); pin(cb); pin(gb); pin(xv); pin(cc); pin(cs);
  };
  auto silu = [](float v) { return v * rcp(1.f + ex2(v * -LOG2E)); };
  auto silu8 = [&](v4u c) {
    v4u r;
#pragma unroll
    for (int j = 0; j < 4; ++j) r[j] = pk2(silu(lo16(c[j])), silu(hi16(c[j])));
    return r;
  };

  if (ww < 4) {
    __builtin_amdgcn_s_setprio(3);
    const int dg = xid >> 6;
    need_ev(1);
    const unsigned t4 = next_tag(__builtin_amdgcn_readfirstlane(*(LAS unsigned*)(L + L_E))) & 15u;
    v4u cv = *(LAS v4u*)(L + L_C + half * 2048 + (128 * (xid >> 5) + 32 * ((n >> 1) & 3) + 8 * g) * 2);
    cv = silu8(cv);
    pin(cv);
    {
      v8bf mf[4];
      read_frags(mf[0], mf[1], 0, 0, 1); read_frags(mf[2], mf[3], 1, 1, 2);
      publish_part(gemv_mfma<4>(mf, cv, lane), rws, WS_MP + xid * 512, t4, w, lane);
    }
    const int hb = g & 1, hp = 2 * n + (lane >> 5);
    float ln0, ln1;
    {
      v4u xr[4];
#pragma unroll
      for (int i = 0; i < 4; ++i) xr[i] = *(LAS v4u*)(L + L_X + hb * 2048 + i * 512 + hp * 16);
      const unsigned xo = *(LAS unsigned*)(L + L_X + hb * 2048 + (256 * dg + 64 * w + 2 * hp) * 2);
      auto bsum = [](float x) { x = red32(x, x); x += dppf<ROR8>(x); x += dppf<ROR4>(x); x += dppf<XOR2>(x); x += dppf<XOR1>(x); return x; };
      float s = 0.f;
#pragma unroll
      for (int i = 0; i < 4; ++i)
#pragma unroll
        for (int j = 0; j < 4; ++j) s += lo16(xr[i][j]) + hi16(xr[i][j]);
      const float m = bsum(s) * (1.f / 1024);
      float q = 0.f;
#pragma unroll
      for (int i = 0; i < 4; ++i)
#pragma unroll
        for (int j = 0; j < 4; ++j) { const float a = lo16(xr[i][j]) - m, b = hi16(xr[i][j]) - m; q += a * a + b * b; }
      const float var = bsum(q) * (1.f / 1024);
      const float mb = rb(m), rden = rcp(rb(__builtin_amdgcn_sqrtf(rb(rb(var) + 1e-5f))));
      ln0 = rb(rb(lo16(xo) - mb) * rden); ln1 = rb(rb(hi16(xo) - mb) * rden);
    }
    pin(ln0); pin(ln1);
    v8bf qw[8];
    {
      v4u ps[8];
      __builtin_amdgcn_s_sleep(SLH);
      gather_tagged<8, 16384, HOLDH>(ps, rws, WS_MP + ((lane >> 5) * 16 + 4 * dg + w) * 512 + (lane & 31) * 16, t4, bg, (LAS unsigned*)(L + L_HOLD), lane, [&]() {
#pragma unroll
        for (int i = 0; i < 4; ++i) read_frags(qw[2 * i], qw[2 * i + 1], 2 + i, 2 + i, 3 + i);
      });
      v4f sm = __builtin_bit_cast(v4f, ps[0]);
#pragma unroll
      for (int i = 1; i < 8; ++i) sm += __builtin_bit_cast(v4f, ps[i]);
      auto s02 = __builtin_amdgcn_permlane32_swap(__float_as_uint(sm[0]), __float_as_uint(sm[2]), false, false);
      auto s13 = __builtin_amdgcn_permlane32_swap(__float_as_uint(sm[1]), __float_as_uint(sm[3]), false, false);
      const unsigned sa = pk2(__uint_as_float(s02[0]), __uint_as_float(s13[0])), ca = pk2(__uint_as_float(s02[1]), __uint_as_float(s13[1]));
      const unsigned hh = pk2(rb(ln0 * rb(1.f + lo16(ca))) + lo16(sa), rb(ln1 * rb(1.f + hi16(ca))) + hi16(sa));
      *(LAS unsigned*)(L + L_H + hb * 512 + w * 128 + hp * 4) = hh;
      (void)arrive(L, L_CNTH, 4, lane);
      wait_l(L_CNTH, 4, S0);
      const v4u hv = *(LAS v4u*)(L + L_H + half * 512 + (n >> 1) * 64 + g * 16);
      publish_part(gemv_mfma<8>(qw, hv, lane), rws, WS_QP + (xid ^ 1) * 512, t4, w, lane);
    }
    constexpr float SCALE = 0.08838834764831845f;
    v4u qf[4];
    float m = -3.0e38f, l = 0.f;
    v4f acc0 = {0.f, 0.f, 0.f, 0.f}, acc1 = {0.f, 0.f, 0.f, 0.f};
    const bool hiN = n >= 8;
    unsigned ko[4];
#pragma unroll
    for (int kk = 0; kk < 4; ++kk) ko[kk] = n * 256 + (((4 * kk + g) ^ n) * 16);
    const int vrow = 4 * g + ((lane >> 2) & 3);
    const unsigned vo0 = vrow * 256 + (((2 * w) ^ (vrow & 7)) * 32) + (lane & 3) * 8;
    const unsigned vo1 = vrow * 256 + (((2 * w + 1) ^ (vrow & 7)) * 32) + (lane & 3) * 8;
    unsigned fv = 0;
    int slot = 6;
    auto need = [&](unsigned k) { need_ev(fv + k + (fv + k > NF0 ? 10 : 6)); };
    auto next_slot = [&]() {
      ++fv;
      LAS char* S = L + L_RING + slot * 8192;
      slot = (slot + 1 == NSLOT) ? 0 : slot + 1;
      return S;
    };
    struct Pend { unsigned p01, p23; float alpha; };
    v4u kf[8];
    v8bf fa, fb;
    v4f s1, s2;
    auto read_k = [&](v4u (&kf)[8]) {
      LAS char* S = next_slot();
#pragma unroll
      for (int kk = 0; kk < 4; ++kk) { kf[2 * kk] = *(LAS v4u*)(S + ko[kk]); kf[2 * kk + 1] = *(LAS v4u*)(S + ko[kk] + 4096); }
    };
    auto read_v = [&]() {
      LAS char* S = next_slot();
      const v4bf va0 = __builtin_amdgcn_ds_read_tr16_b64_v4bf16((LAS v4bf*)(S + vo0));
      const v4bf va1 = __builtin_amdgcn_ds_read_tr16_b64_v4bf16((LAS v4bf*)(S + vo0 + 4096));
      const v4bf vb0 = __builtin_amdgcn_ds_read_tr16_b64_v4bf16((LAS v4bf*)(S + vo1));
      const v4bf vb1 = __builtin_amdgcn_ds_read_tr16_b64_v4bf16((LAS v4bf*)(S + vo1 + 4096));
      fa = __builtin_shufflevector(va0, va1, 0, 1, 2, 3, 4, 5, 6, 7); fb = __builtin_shufflevector(vb0, vb1, 0, 1, 2, 3, 4, 5, 6, 7);
    };
    auto qk = [&](const v4u (&kf)[8]) {
      s1 = (v4f){0.f, 0.f, 0.f, 0.f}; s2 = (v4f){0.f, 0.f, 0.f, 0.f};
#pragma unroll
      for (int kk = 0; kk < 4; ++kk) {
        s1 = __builtin_amdgcn_mfma_f32_16x16x32_bf16(__builtin_bit_cast(v8bf, kf[2 * kk]), __builtin_bit_cast(v8bf, qf[kk]), s1, 0, 0, 0);
        s2 = __builtin_amdgcn_mfma_f32_16x16x32_bf16(__builtin_bit_cast(v8bf, kf[2 * kk + 1]), __builtin_bit_cast(v8bf, qf[kk]), s2, 0, 0, 0);
      }
      __builtin_amdgcn_sched_barrier(0);
    };
    auto softmax = [&](auto last) {
      const unsigned u0 = pk2(hiN ? s2[0] : s1[0], hiN ? s2[1] : s1[1]), u1 = pk2(hiN ? s2[2] : s1[2], hiN ? s2[3] : s1[3]);
      const unsigned z0 = pk2(lo16(u0) * SCALE, hi16(u0) * SCALE), z1 = pk2(lo16(u1) * SCALE, hi16(u1) * SCALE);
      const float sc[4] = {lo16(z0), hi16(z0), lo16(z1), hi16(z1)};
      float mn = fmx(m, max32(max16(fmx(fmx3(sc[0], sc[1], sc[2]), sc[3]))));
      if constexpr (decltype(last)::value) mn = max8(mn);
      Pend r;
      r.alpha = ex2((m - mn) * LOG2E);
      m = mn;
      float pj[4];
#pragma unroll
      for (int j = 0; j < 4; ++j) pj[j] = ex2((sc[j] - mn) * LOG2E);
      l = l * r.alpha + ((pj[0] + pj[1]) + (pj[2] + pj[3]));
      r.p01 = pk2(pj[0], pj[1]); r.p23 = pk2(pj[2], pj[3]);
      pin(r.p01); pin(r.p23); pin(r.alpha); pin(l);
      return r;
    };
    auto scale_acc = [&](const Pend& r, v4u& pf) {
      pf = (v4u){hiN ? 0u : r.p01, hiN ? 0u : r.p23, hiN ? r.p01 : 0u, hiN ? r.p23 : 0u};
      acc0 *= r.alpha; acc1 *= r.alpha;
      pin(pf); pin(acc0); pin(acc1);
    };
    auto pv = [&](v4u pf) {
      acc0 = __builtin_amdgcn_mfma_f32_16x16x32_bf16(fa, __builtin_bit_cast(v8bf, pf), acc0, 0, 0, 0);
      acc1 = __builtin_amdgcn_mfma_f32_16x16x32_bf16(fb, __builtin_bit_cast(v8bf, pf), acc1, 0, 0, 0);
    };
    auto release = [&]() { WAIT_LGKM0(); *lw(L_PROG + w * 4) = 6 + fv; };
    v4u k0f[8], k1f[8];
    need(4); read_k(k0f); read_k(k1f);
    WAIT_LGKM0();
    read_k(kf); read_v();
    release();
    pin(k0f); pin(k1f); pin(kf); pin(fa); pin(fb);
    {
      v4u ps[4];
      __builtin_amdgcn_s_sleep(SLQ);
      gather_tagged<4, 32768, HOLDQ>(ps, rws, WS_QP + (16 * (grp & 3) + 4 * w + g) * 512 + (grp >> 2) * 256 + n * 16, t4, bg, (LAS unsigned*)(L + L_HOLD), lane, []() {});
      const v4f sm = (__builtin_bit_cast(v4f, ps[0]) + __builtin_bit_cast(v4f, ps[1])) + (__builtin_bit_cast(v4f, ps[2]) + __builtin_bit_cast(v4f, ps[3]));
      const v2u qq = {pk2(sm[0], sm[1]), pk2(sm[2], sm[3])};
      *(LAS v2u*)(L + L_Q + w * 512 + lane * 8) = qq;
      (void)arrive(L, L_CNTG, 4, lane);
      wait_l(L_CNTG, 4, S0);
#pragma unroll
      for (int kk = 0; kk < 4; ++kk) qf[kk] = *(LAS v4u*)(L + L_Q + (n & 7) * 256 + kk * 64 + g * 16);
    }
    unsigned rec, o1, o2;
    {
      v4u pf;
      qk(k0f);
      Pend pa = softmax(std::false_type{});
      qk(k1f);
      Pend pb = softmax(std::false_type{});
      need(1);
      qk(kf);
      read_k(kf);
      __builtin_amdgcn_sched_barrier(0);
      scale_acc(pa, pf);
      pa = pb;
      pb = softmax(std::false_type{});
      release();
      pv(pf);
#pragma unroll 1
      for (int t = 1; t < NT - 3; ++t) {
        if (t == NT - 8 && fin && __builtin_amdgcn_readfirstlane(*lw(L_FC))) load_mr();
        need(2);
        qk(kf);
        read_v(); read_k(kf);
        __builtin_amdgcn_sched_barrier(0);
        scale_acc(pa, pf);
        pa = pb;
        pb = softmax(std::false_type{});
        release();
        pv(pf);
      }
      qk(kf);
      scale_acc(pa, pf);
      const Pend pc = softmax(std::true_type{});
      l = red32(l, l); l = red16(l, l); l += dppf<ROR8>(l);
      rec = WS_PART + ((grp * 32 + (n & 7) * 4 + w) * 32 + (bid & 31)) * 128 + (n >> 3) * 64 + g * 16;
      o1 = WS_PART + ((bid + w) * 32 + (lane >> 3)) * 128 + (lane & 7) * 16;
      o2 = WS_OS + (bid + w) * 64 + (lane & 7) * 8 + (lane >> 5) * 4;
      pin(l); pin(m); pin(rec); pin(o1); pin(o2);
      wait_l(L_FC, 1, S0);
      need(1); read_v(); pv(pf);
      scale_acc(pb, pf); need(1); read_v(); pv(pf);
      scale_acc(pc, pf); need(1); read_v(); pv(pf);
    }
    {
      float o[4];
#pragma unroll
      for (int j = 0; j < 4; ++j) o[j] = (hiN ? acc1[j] : acc0[j]) + dppf<ROR8>(hiN ? acc0[j] : acc1[j]);
      st128(rws, (v4u){nosent(pk2(o[0], o[1])), nosent(pk2(o[2], o[3])), __float_as_uint(m), __float_as_uint(l)}, rec);
    }
    if (w < 2 && !(xid & 1)) {
      v4u po[4];
      __builtin_amdgcn_s_sleep(S1A);
      bool ok = false;
      for (;; --bg) {
        CBAR();
        if (!ok) {
#pragma unroll
          for (int i = 0; i < 4; ++i) po[i] = ld128<AUX_SC1>(rws, o1, i * 1024);
        }
        ok = max(max(umax4(po[0]), umax4(po[1])), max(umax4(po[2]), umax4(po[3]))) != SENT;
        if (__all(ok) || bg == 0) break;
      }
      const float M = max8(max16(max32(fmx(fmx3(__uint_as_float(po[0].z), __uint_as_float(po[1].z), __uint_as_float(po[2].z)), __uint_as_float(po[3].z)))));
      float Ls = 0.f, o4[4] = {0.f, 0.f, 0.f, 0.f};
#pragma unroll
      for (int i = 0; i < 4; ++i) {
        const float wg = ex2((__uint_as_float(po[i].z) - M) * LOG2E);
        Ls = fmaf(wg, __uint_as_float(po[i].w), Ls);
        o4[0] = fmaf(wg, lo16(po[i].x), o4[0]); o4[1] = fmaf(wg, hi16(po[i].x), o4[1]);
        o4[2] = fmaf(wg, lo16(po[i].y), o4[2]); o4[3] = fmaf(wg, hi16(po[i].y), o4[3]);
      }
      Ls = red32(Ls, Ls); Ls = red16(Ls, Ls); Ls += dppf<ROR8>(Ls);
      float o = red16(red32(o4[0], o4[2]), red32(o4[1], o4[3]));
      o += dppf<ROR8>(o);
      o *= rcp(Ls);
      auto sw = __builtin_amdgcn_permlane16_swap(__float_as_uint(o), __float_as_uint(o), false, false);
      const unsigned pr = nosent(pk2(__uint_as_float(sw[0]), __uint_as_float(sw[1])));
      if ((lane & 24) == 0) st32(rws, pr, o2);
      *lw(L_FO) = 1u;
    }
    if (fin) final_prep();
  } else {
    const int kg = xid >> 5, ng = (xid >> 1) & 15;
    unsigned xo = WS_XP + (lane >> 5) * 16384 + kg * 2048 + ng * 128 + w * 32 + (lane & 14) * 2;
    wait_l(L_HALV, 1, S4);
    {
      const v4u s4 = {SENT, SENT, SENT, SENT};
      const int rec = (grp * 32 + (n & 7) * 4 + w) * 32 + (bid & 31);
      st128(rws, s4, WS_PART + rec * 128 + (n >> 3) * 64 + (lane >> 4) * 16);
      if ((lane & 17) == 0 && (xid & 1)) st32(rws, SENT, xo);
      if (w == 3) {
        const int i4 = lane >> 2, j4 = lane & 3;
        unsigned o = WS_MR + (j4 >> 1) * 8192 + xid * 32 + (j4 & 1) * 16;
        if (i4) o = WS_OS + bid * 64 + (lane - 4) * 16;
        if (lane < ((xid & 1) ? 4 : 12)) st128(rws, s4, o);
        if (lane < TAB_PER / 2) st32(rws, SENT, WS_TAB + xid * (TAB_PER * 2) + lane * 4);
      }
    }
    unsigned n1[7], n2[7], n3[7];
#pragma unroll
    for (int i = 0; i < 7; ++i) {
      const unsigned o = 1792 * w + 4 * lane + 256 * i;
      n1[i] = ld32<0>(rw1, o); n2[i] = ld32<0>(rb1, o); n3[i] = ld32<0>(rw2, o);
    }
    const unsigned b2r = __builtin_amdgcn_raw_buffer_load_b16(rb2, 0, 0, 0);
    const unsigned tag = next_tag(__builtin_amdgcn_readfirstlane(*(LAS unsigned*)(L + L_E)));
    v4u cv = silu8(*(LAS v4u*)(L + L_C + half * 2048 + (256 * (xid >> 6) + 32 * (n >> 1) + 8 * g) * 2));
    pin(cv);
    v2f ta[TAB_PER / 2], sa[TAB_PER / 2];
#pragma unroll
    for (int j = 0; j < TAB_PER / 2; ++j) {
      const unsigned i0 = xid * TAB_PER + 2 * j;
      const unsigned bits = (i0 >= TAB_HALF ? 0x8000u + (i0 - TAB_HALF) : i0) + TAB_BASE;
      ta[j] = (v2f){i0 == 0 ? 0.f : lo16(bits), lo16(bits + 1)};
      sa[j] = (v2f){0.f, 0.f};
    }
    auto table = [&](int steps) {
#pragma unroll 1
      for (int i = 0; i < steps; ++i) {
        const Unit u0 = mk_unit(lo16(n1[0]), lo16(n2[0]), lo16(n3[0])), u1 = mk_unit(hi16(n1[0]), hi16(n2[0]), hi16(n3[0]));
#pragma unroll
        for (int k = 0; k < 6; ++k) { n1[k] = n1[k + 1]; n2[k] = n2[k + 1]; n3[k] = n3[k + 1]; }
#pragma unroll
        for (int j = 0; j < TAB_PER / 2; ++j) sa[j] = unit_acc(u1, ta[j], unit_acc(u0, ta[j], sa[j]));
      }
    };
    table(TSPLIT);
    wait_vm<0>();
    (void)arrive(L, L_CNTC, 4, lane);

    v8bf wf[16];
#pragma unroll
    for (int i = 0; i < 4; ++i) read_frags(wf[2 * i], wf[2 * i + 1], 6 + NF0 + i, 2 + i, i + 1);
    publish_part(gemv_mfma<8>(wf, cv, lane), rws, WS_RP + (xid ^ 2) * 512, tag & 15u, w, lane);
    table(7 - TSPLIT);
    {
      float a[10];
#pragma unroll
      for (int j = 0; j < 10; ++j) a[j] = red32(sa[j][0], sa[j][1]);
#pragma unroll
      for (int j = 0; j < 5; ++j) {
        a[j] = red16(a[j], a[5 + j]);
        a[j] += dppf<ROR8>(a[j]); a[j] += dppf<ROR4>(a[j]); a[j] += dppf<XOR2>(a[j]); a[j] += dppf<XOR1>(a[j]);
        if ((lane & 15) == 0) *(LAS float*)(L + L_REDT + w * 128 + (2 * (j + 5 * ((lane >> 4) & 1)) + (lane >> 5)) * 4) = a[j];
      }

      if (arrive(L, L_CNTT, 4, lane)) {
        float s = 0.f;
#pragma unroll
        for (int i = 0; i < 4; ++i) s += *(LAS float*)(L + L_REDT + i * 128 + (lane & 31) * 4);
        const float y = rb(s) + lo16(b2r);
        const unsigned pr = nosent(pk2(y, dppf<XOR1>(y)));
        if (lane < TAB_PER && !(lane & 1)) st32(rws, pr, WS_TAB + xid * (TAB_PER * 2) + lane * 2);
      }
    }
    if (w == 1) {
      wait_l(L_CNTC, 4, S4);
      v4u ps[1];
      gather_tagged<1, 0, 0>(ps, rws, WS_RP + (((lane >> 3) & 3) * 64 + (xid >> 2)) * 512 + ((lane >> 2) & 1) * 256 + (xid & 3) * 64 + (lane & 3) * 16, tag & 15u, bg, nullptr, lane, []() {});
      const v4u v = ps[0];
      float s[4];
#pragma unroll
      for (int j = 0; j < 4; ++j) { s[j] = __uint_as_float(v[j]); s[j] += dppf<ROR8>(s[j]); s[j] = red16(s[j], s[j]); }
      const v2u o = {nosent(pk2(s[0], s[1])), nosent(pk2(s[2], s[3]))};
      if (lane < 8) st64(rws, o, WS_MR + (lane >> 2) * 8192 + xid * 32 + (lane & 3) * 8);
    }
    if (fin) {
      wait_l(L_HALV, NEV - 24, S4);
      wait_l(L_FC, 1, S4);
      load_mr();
#pragma unroll 1
      for (int i = w; i < 10; i += 4) {
        v4u v;
        for (;; --bg) { CBAR(); v = ld128<AUX_SC1>(rws, WS_TAB + i * 1024 + lane * 16); if (__all(umax4(v) != SENT) || bg == 0) break; }
        *(LAS v4u*)(L + L_TAB + i * 1024 + lane * 16) = v;
      }
      final_prep();
    }
    if (xid & 1) {
      unsigned oo = WS_OS + half * 8192 + (512 * kg + 32 * (n >> 1) + 8 * g) * 2;
      pin(xo); pin(oo);
      wait_l(L_HALV, NEV, S4);
      wait_l(L_FC, 1, S0);
#pragma unroll
      for (int i = 0; i < 8; ++i) read_frags(wf[2 * i], wf[2 * i + 1], NEV + i, (2 * NT + 6 + i) % NSLOT, 5 + i);
      v4u ov, ov2;
      bool ok = false;
      for (;; --bg) {
        CBAR();
        if (!ok) { ov = ld128<AUX_SC1>(rws, oo); ov2 = ld128<AUX_SC1>(rws, oo + 512); }
        ok = max(umax4(ov), umax4(ov2)) != SENT;
        if (__all(ok) || bg == 0) break;
      }
      const float d0 = gemv_mfma<16>(wf, ov, lane, ov2);
      const unsigned pr = nosent(pk2(d0, dppf<XOR1>(d0)));
      if ((lane & 17) == 0) st32(rws, pr, xo);
    }
  }
  if (!fin) return;

  v4u sel;
#pragma unroll
  for (int j = 0; j < 4; ++j) sel[j] = ((lane & 7) >> 1) == j ? ((lane & 1) ? 0x3F800000u : 0x00003F80u) : 0u;
  unsigned xpo = WS_XP + bb * 16384 + (lane >> 4) * 2048 + ww * 256 + (lane & 15) * 16;
  unsigned sti = L_ST + (lane >> 3) * 64 + (lane & 7) * 4, outo = (bb * 1024 + nA) * 2;
  pin(sel); pin(xpo); pin(sti); pin(sto); pin(outo);
  wait_l(L_FO, 1, S1);
  __builtin_amdgcn_s_sleep(FSL);
  v2f x1;
  {
    v4u ps[2];
    bool ok = false;
    for (;; --bg) {
      CBAR();
      if (!ok) { ps[0] = ld128<AUX_SC1>(rws, xpo); ps[1] = ld128<AUX_SC1>(rws, xpo, 8192); }
      ok = max(umax4(ps[0]), umax4(ps[1])) != SENT;
      if (__all(ok) || bg == 0) break;
    }
    v4f d = {0.f, 0.f, 0.f, 0.f};
#pragma unroll
    for (int i = 0; i < 2; ++i) d = __builtin_amdgcn_mfma_f32_16x16x32_bf16(__builtin_bit_cast(v8bf, sel), __builtin_bit_cast(v8bf, ps[i]), d, 0, 0, 0);
    const bool up = lane >= 32;
    x1 = rbv(xv + rbv(ga * rbv((v2f){up ? d[2] : d[0], up ? d[3] : d[1]})));
  }
  {
    const v2f u = x1 * cc, a = x1 * x1, b = x1 * cb, c = x1 * u, e = x1 * cs;
    wave_sums(x1[0] + x1[1], a[0] + a[1], b[0] + b[1], c[0] + c[1], u[0] + u[1], e[0] + e[1], 0);
  }
  WAIT_LGKM0(); __builtin_amdgcn_s_barrier(); CBAR();
  unsigned tp;
  {
    float qa = *(LAS float*)(L + sti), qb = *(LAS float*)(L + sti + 32);
    float q = red32(qa, qb);
    q = red16(q, q); q += dppf<ROR8>(q);
    q *= (1.f / 1024);
    auto rl = [&](int i) { return __uint_as_float(__builtin_amdgcn_readlane(__float_as_uint(q), i)); };
    const float ex = rl(0), exx = rl(1), exc = rl(2), exxcc = rl(3), excc = rl(4), excs = rl(5);
    const float ec = rl(32), es = rl(33), ecc = rl(34), ecs = rl(35), ess = rl(36);
    const float mb = rb(ex), r1 = rcp(rb(__builtin_amdgcn_sqrtf(rb(rb(fmaxf(fmaf(-ex, ex, exx), 0.f)) + 1e-5f))));
    const float eh = fmaf(r1, fmaf(-mb, ec, exc), es);
    const float ehh = fmaf(r1 * r1, fmaf(mb, fmaf(mb, ecc, -2.f * excc), exxcc), fmaf(2.f * r1, fmaf(-mb, ecs, excs), ess));
    const float mb2 = rb(eh), r2 = rcp(rb(__builtin_amdgcn_sqrtf(rb(rb(fmaxf(fmaf(-eh, eh, ehh), 0.f)) + 1e-5f))));
    const v2f h = rbv(rbv(rbv(rbv(x1 - mb) * r1) * cb) + sb);
    const v2f t = rbv(h - mb2) * r2;
    tp = pk2(t[0], t[1]);
  }
  const unsigned u0 = tp & 0xffffu, u1 = tp >> 16;
  const float t0 = lo16(tp), t1 = hi16(tp);
  const unsigned i0 = (u0 & 0x7fffu) - TAB_BASE, i1 = (u1 & 0x7fffu) - TAB_BASE;
  const bool tb0 = i0 < (unsigned)TAB_HALF && u0 != TAB_BASE, tb1 = i1 < (unsigned)TAB_HALF && u1 != TAB_BASE;
  const bool in0 = tb0 || t0 == 0.f, in1 = tb1 || t1 == 0.f;
  float y0 = lo16(*(LAS u16*)(L + L_TAB + (tb0 ? i0 + (u0 >> 15) * TAB_HALF : 0u) * 2));
  float y1 = lo16(*(LAS u16*)(L + L_TAB + (tb1 ? i1 + (u1 >> 15) * TAB_HALF : 0u) * 2));
  for (u64 miss = __ballot(!(in0 && in1)); miss; miss &= miss - 1) {
    const int src = __builtin_ctzll(miss);
    const v2f t = {__uint_as_float(__builtin_amdgcn_readlane(__float_as_uint(t0), src)), __uint_as_float(__builtin_amdgcn_readlane(__float_as_uint(t1), src))};
    v2f s = {0.f, 0.f};
#pragma unroll 1
    for (int i = 0; i < 28; ++i) {
      const unsigned o = 256 * i + 4 * lane, n1 = ld32<0>(rw1, o), n2 = ld32<0>(rb1, o), n3 = ld32<0>(rw2, o);
      s = unit_acc(mk_unit(hi16(n1), hi16(n2), hi16(n3)), t, unit_acc(mk_unit(lo16(n1), lo16(n2), lo16(n3)), t, s));
    }
    const float b2f = lo16(__builtin_amdgcn_raw_buffer_load_b16(rb2, 0, 0, 0));
    const float z0 = rb(rb(allsum64(s[0])) + b2f), z1 = rb(rb(allsum64(s[1])) + b2f);
    if (lane == src) { y0 = z0; y1 = z1; }
  }
  const v2f r = x1 + rbv(gb * (v2f){y0, y1});
  __builtin_amdgcn_raw_buffer_store_b32(pk2(r[0], r[1]), mk_rsrc(p.out, 4096), outo, 0, 0);
  asm volatile("fb_code2:");
}

extern "C" void kernel_launch(void* const* d_in, const int* in_sizes, int n_in, void* d_out, int out_size, void* d_ws,
                              size_t ws_size, hipStream_t stream) {
  Params p;
  p.x = (const char*)d_in[0]; p.K = (const char*)d_in[1]; p.V = (const char*)d_in[2]; p.wq = (const char*)d_in[3];
  p.wo = (const char*)d_in[4]; p.w1 = (const char*)d_in[6]; p.b1 = (const char*)d_in[7]; p.w2 = (const char*)d_in[8];
  p.b2 = (const char*)d_in[9]; p.c = (const char*)d_in[10]; p.wm = (const char*)d_in[11];
  p.out = (char*)d_out; p.ws = (char*)d_ws;
  fused_block<<<NWG, NTHR, 0, stream>>>(p);
}
```
